```python
import torch

def _softmax(x, axis=-1):
    return torch.softmax(x, dim=axis)

def _gelu(x):
    return torch.nn.functional.gelu(x, approximate='tanh')

def _silu(x):
    return torch.nn.functional.silu(x)

def _sigmoid(x):
    return torch.sigmoid(x)

def _layernorm(x, axis=-1, eps=1e-05):
    m = x.mean(dim=axis, keepdim=True)
    v = x.var(dim=axis, keepdim=True, unbiased=False)
    return (x - m) / torch.sqrt(v + eps)

def _scatter_add(tgt, idx, src, axis=0):
    return tgt.clone().index_add_(axis, idx, src)

def _segment_sum(src, seg, n):
    out = torch.zeros((n,) + src.shape[1:], dtype=src.dtype)
    return out.index_add_(0, seg, src)

def _pad(x, pw):
    flat = tuple((v for pair in reversed(pw) for v in pair))
    return torch.nn.functional.pad(x, flat)
kernel_name = 'adaLN_cond__sliding_block_cached__mixer_channel_mlp__decode'

def setup_inputs():

    _w = lambda a: (lambda t: t.to(torch.bfloat16) if t.is_floating_point() else t)(torch.as_tensor(a))

    def _A_setup():
        B, Sq, D = (2, 1, 512)
        Hq, Hkv, Sk, Dh = (32, 8, 8192, 128)
        assert Hq % Hkv == 0, (Hq, Hkv)
        return {'x': _w((lambda *s: torch.randn(*s, dtype=torch.bfloat16) * 0.1)(B, Sq, D)), 'k_cache': _w((lambda *s: torch.randn(*s, dtype=torch.bfloat16) * 0.8)(B, Hkv, Sk, Dh)), 'v_cache': _w((lambda *s: torch.randn(*s, dtype=torch.bfloat16) * 0.8)(B, Hkv, Sk, Dh)), 'wq': _w((lambda *s: torch.randn(*s, dtype=torch.bfloat16) * 0.8)(D, Hq * Dh) * D ** (-0.5)), 'wo': _w((lambda *s: torch.randn(*s, dtype=torch.bfloat16) * 0.8)(Hq * Dh, D) * (Hq * Dh) ** (-0.5))}
    _A_in = _A_setup()

    def _B_setup():
        B, S, D, F = (2, 1, 512, 1792)
        return {'x': _w((lambda *s: torch.randn(*s, dtype=torch.bfloat16) * 0.1)(B, S, D)), 'w1': _w((lambda *s: torch.randn(*s, dtype=torch.bfloat16) * 3.2)(D, F) * D ** (-0.5)), 'b1': _w((lambda *s: torch.randn(*s, dtype=torch.bfloat16) * 3.2)(F)), 'w2': _w((lambda *s: torch.randn(*s, dtype=torch.bfloat16) * 3.2)(F, D) * F ** (-0.5)), 'b2': _w((lambda *s: torch.randn(*s, dtype=torch.bfloat16) * 3.2)(D))}
    _B_in = _B_setup()
    return {'A_x': _A_in['x'], 'A_k_cache': _A_in['k_cache'], 'A_v_cache': _A_in['v_cache'], 'A_wq': _A_in['wq'], 'A_wo': _A_in['wo'], 'B_x': _B_in['x'], 'B_w1': _B_in['w1'], 'B_b1': _B_in['b1'], 'B_w2': _B_in['w2'], 'B_b2': _B_in['b2'], 'c': _w((lambda *s: torch.randn(*s, dtype=torch.bfloat16) * 0.4)(2, 512) * 8.0), 'w_mod': _w((lambda *s: torch.randn(*s, dtype=torch.bfloat16) * 0.4)(512, 6 * 512) * 512 ** (-0.5) * 8.0)}

def reference(A_x, A_k_cache, A_v_cache, A_wq, A_wo, B_x, B_w1, B_b1, B_w2, B_b2, c, w_mod):

    def _A_ref(x, k_cache, v_cache, wq, wo):
        B, Sq, D = x.shape
        Hq, Dh = (32, 128)
        Hkv, Sk = (k_cache.shape[1], k_cache.shape[2])
        W = min(4096, max(256, Sk // 4))
        rep = Hq // Hkv
        q = (lambda x, *d: x.permute(*d))((x @ wq).reshape(B, Sq, Hq, Dh), 0, 2, 1, 3)
        k = (lambda x, n, axis: torch.repeat_interleave(x, n, dim=axis))(k_cache[:, :, Sk - W:, :], rep, axis=1)
        v = (lambda x, n, axis: torch.repeat_interleave(x, n, dim=axis))(v_cache[:, :, Sk - W:, :], rep, axis=1)
        scores = torch.einsum('bhqd,bhkd->bhqk', q, k) * Dh ** (-0.5)
        p = _softmax(scores, axis=-1)
        o = torch.einsum('bhqk,bhkd->bhqd', p, v)
        return (lambda x, *d: x.permute(*d))(o, 0, 2, 1, 3).reshape(B, Sq, Hq * Dh) @ wo

    def _B_ref(x, w1, b1, w2, b2):
        y = _layernorm(x, axis=-1)
        h = _gelu(torch.einsum('bsd,df->bsf', y, w1) + b1)
        return torch.einsum('bsf,fd->bsd', h, w2) + b2
    x = A_x
    mod = (_silu(c) @ w_mod).reshape(c.shape[0], 6, 1, -1)
    sa, ca, ga, sb, cb, gb = (mod[:, 0].clone(), mod[:, 1].clone(), mod[:, 2].clone(), mod[:, 3].clone(), mod[:, 4].clone(), mod[:, 5].clone())
    h = _layernorm(x, axis=-1) * (1 + ca) + sa
    x = x + ga * _A_ref(h, A_k_cache, A_v_cache, A_wq, A_wo)
    h = _layernorm(x, axis=-1) * (1 + cb) + sb
    x = x + gb * _B_ref(h, B_w1, B_b1, B_w2, B_b2)
    return x
```

```cpp
#include <hip/hip_runtime.h>
#include <hip/hip_bf16.h>
#include <stdint.h>

typedef unsigned short u16;
__device__ __forceinline__ float bf2f(u16 b) { return __uint_as_float(((unsigned)b) << 16); }
__device__ __forceinline__ u16 f2bf(float f) { unsigned u = __float_as_uint(f); u += 0x7fffu + ((u >> 16) & 1u); return (u16)(u >> 16); }

constexpr int NB = 2, D = 512, HQ = 32, HKV = 8, SK = 8192, DH = 128, W = 2048, F = 1792;

__device__ float block_sum(float v, float* sm) {
  for (int o = 32; o > 0; o >>= 1) v += __shfl_xor(v, o);
  int w = threadIdx.x >> 6;
  __syncthreads();
  if ((threadIdx.x & 63) == 0) sm[w] = v;
  __syncthreads();
  float s = 0.f;
  for (int i = 0; i < (int)(blockDim.x >> 6); ++i) s += sm[i];
  return s;
}

template <int K, int N>
__device__ void gemv_cols(const float* xin  , const u16* w, float* res0, float* res1, float* sm) {
  int col = blockIdx.x * 64 + (threadIdx.x & 63);
  int kg = threadIdx.x >> 6;
  float a0 = 0.f, a1 = 0.f;
  for (int k = kg; k < K; k += 4) {
    float wv = bf2f(w[(size_t)k * N + col]);
    a0 += xin[k] * wv; a1 += xin[K + k] * wv;
  }
  __syncthreads();
  sm[threadIdx.x] = a0; sm[256 + threadIdx.x] = a1;
  __syncthreads();
  int c = threadIdx.x & 63;
  *res0 = sm[c] + sm[64 + c] + sm[128 + c] + sm[192 + c];
  *res1 = sm[256 + c] + sm[320 + c] + sm[384 + c] + sm[448 + c];
}

__global__ void k_mod(const u16* c, const u16* wmod, float* mod) {
  __shared__ float xin[2 * D]; __shared__ float sm[512];
  for (int i = threadIdx.x; i < 2 * D; i += 256) { float v = bf2f(c[i]); xin[i] = v / (1.f + __expf(-v)); }
  __syncthreads();
  float r0, r1; gemv_cols<D, 6 * D>(xin, wmod, &r0, &r1, sm);
  if (threadIdx.x < 64) { int col = blockIdx.x * 64 + threadIdx.x; mod[col] = r0; mod[6 * D + col] = r1; }
}

__device__ void ln_inplace(float* x, float* sm) {
  for (int b = 0; b < 2; ++b) {
    float s = 0.f; for (int i = threadIdx.x; i < D; i += 256) s += x[b * D + i];
    float m = block_sum(s, sm) / D;
    float v = 0.f; for (int i = threadIdx.x; i < D; i += 256) { float d = x[b * D + i] - m; v += d * d; }
    float var = block_sum(v, sm) / D;
    float r = 1.f / sqrtf(var + 1e-5f);
    for (int i = threadIdx.x; i < D; i += 256) x[b * D + i] = (x[b * D + i] - m) * r;
    __syncthreads();
  }
}

__global__ void k_q(const u16* x, const float* mod, const u16* wq, float* q) {
  __shared__ float xin[2 * D]; __shared__ float sm[512];
  for (int i = threadIdx.x; i < 2 * D; i += 256) xin[i] = bf2f(x[i]);
  __syncthreads();
  ln_inplace(xin, sm);
  for (int i = threadIdx.x; i < 2 * D; i += 256) { int b = i / D, d = i % D; xin[i] = xin[i] * (1.f + mod[b * 6 * D + D + d]) + mod[b * 6 * D + d]; }
  __syncthreads();
  float r0, r1; gemv_cols<D, HQ * DH>(xin, wq, &r0, &r1, sm);
  if (threadIdx.x < 64) { int col = blockIdx.x * 64 + threadIdx.x; q[col] = r0; q[HQ * DH + col] = r1; }
}

__global__ void k_attn(const float* q, const u16* kc, const u16* vc, float* o) {
  __shared__ float qs[DH]; __shared__ float p[W]; __shared__ float sm[512];
  int h = blockIdx.x, b = blockIdx.y, kvh = h / 4;
  if (threadIdx.x < DH) qs[threadIdx.x] = q[b * HQ * DH + h * DH + threadIdx.x];
  __syncthreads();
  const u16* kb = kc + ((size_t)(b * HKV + kvh) * SK + (SK - W)) * DH;
  const u16* vb = vc + ((size_t)(b * HKV + kvh) * SK + (SK - W)) * DH;
  float mx = -INFINITY;
  for (int k = threadIdx.x; k < W; k += 256) {
    float s = 0.f; for (int d = 0; d < DH; ++d) s += qs[d] * bf2f(kb[(size_t)k * DH + d]);
    s *= 0.08838834764831845f; p[k] = s; mx = fmaxf(mx, s);
  }
  for (int of = 32; of > 0; of >>= 1) mx = fmaxf(mx, __shfl_xor(mx, of));
  __syncthreads();
  if ((threadIdx.x & 63) == 0) sm[threadIdx.x >> 6] = mx;
  __syncthreads();
  mx = fmaxf(fmaxf(sm[0], sm[1]), fmaxf(sm[2], sm[3]));
  float l = 0.f;
  for (int k = threadIdx.x; k < W; k += 256) { float e = __expf(p[k] - mx); p[k] = e; l += e; }
  l = block_sum(l, sm);
  __syncthreads();
  int d = threadIdx.x & 127, half = threadIdx.x >> 7;
  float a = 0.f;
  for (int k = half; k < W; k += 2) a += p[k] * bf2f(vb[(size_t)k * DH + d]);
  __syncthreads();
  sm[threadIdx.x] = a;
  __syncthreads();
  if (threadIdx.x < 128) o[b * HQ * DH + h * DH + d] = (sm[d] + sm[128 + d]) / l;
}

__global__ void k_wo(const float* o, const u16* wo, const u16* x, const float* mod, float* x1) {
  __shared__ float xin[2 * HQ * DH]; __shared__ float sm[512];
  for (int i = threadIdx.x; i < 2 * HQ * DH; i += 256) xin[i] = o[i];
  __syncthreads();
  float r0, r1; gemv_cols<HQ * DH, D>(xin, wo, &r0, &r1, sm);
  if (threadIdx.x < 64) { int col = blockIdx.x * 64 + threadIdx.x;
    x1[col] = bf2f(x[col]) + mod[2 * D + col] * r0; x1[D + col] = bf2f(x[D + col]) + mod[6 * D + 2 * D + col] * r1; }
}

__global__ void k_w1(const float* x1, const float* mod, const u16* w1, const u16* b1, float* h1) {
  __shared__ float xin[2 * D]; __shared__ float sm[512];
  for (int i = threadIdx.x; i < 2 * D; i += 256) xin[i] = x1[i];
  __syncthreads();
  ln_inplace(xin, sm);
  for (int i = threadIdx.x; i < 2 * D; i += 256) { int b = i / D, d = i % D; xin[i] = xin[i] * (1.f + mod[b * 6 * D + 4 * D + d]) + mod[b * 6 * D + 3 * D + d]; }
  __syncthreads();
  ln_inplace(xin, sm);
  float r0, r1; gemv_cols<D, F>(xin, w1, &r0, &r1, sm);
  if (threadIdx.x < 64) { int col = blockIdx.x * 64 + threadIdx.x;
    float bb = bf2f(b1[col]);
    float u0 = r0 + bb, u1 = r1 + bb;
    h1[col] = 0.5f * u0 * (1.f + tanhf(0.7978845608028654f * (u0 + 0.044715f * u0 * u0 * u0)));
    h1[F + col] = 0.5f * u1 * (1.f + tanhf(0.7978845608028654f * (u1 + 0.044715f * u1 * u1 * u1))); }
}

__global__ void k_w2(const float* h1, const u16* w2, const u16* b2, const float* x1, const float* mod, u16* out) {
  __shared__ float xin[2 * F]; __shared__ float sm[512];
  for (int i = threadIdx.x; i < 2 * F; i += 256) xin[i] = h1[i];
  __syncthreads();
  float r0, r1; gemv_cols<F, D>(xin, w2, &r0, &r1, sm);
  if (threadIdx.x < 64) { int col = blockIdx.x * 64 + threadIdx.x;
    float bb = bf2f(b2[col]);
    out[col] = f2bf(x1[col] + mod[5 * D + col] * (r0 + bb));
    out[D + col] = f2bf(x1[D + col] + mod[6 * D + 5 * D + col] * (r1 + bb)); }
}

extern "C" void kernel_launch(void* const* d_in, const int* in_sizes, int n_in, void* d_out, int out_size, void* d_ws, size_t ws_size, hipStream_t stream) {
  const u16* x = (const u16*)d_in[0]; const u16* kc = (const u16*)d_in[1]; const u16* vc = (const u16*)d_in[2];
  const u16* wq = (const u16*)d_in[3]; const u16* wo = (const u16*)d_in[4];
  const u16* w1 = (const u16*)d_in[6]; const u16* b1 = (const u16*)d_in[7]; const u16* w2 = (const u16*)d_in[8]; const u16* b2 = (const u16*)d_in[9];
  const u16* c = (const u16*)d_in[10]; const u16* wmod = (const u16*)d_in[11];
  float* ws = (float*)d_ws;
  float* mod = ws; float* q = ws + 8192; float* o = q + 8192; float* x1 = o + 8192; float* h1 = x1 + 8192;
  k_mod<<<6 * D / 64, 256, 0, stream>>>(c, wmod, mod);
  k_q<<<HQ * DH / 64, 256, 0, stream>>>(x, mod, wq, q);
  k_attn<<<dim3(HQ, NB), 256, 0, stream>>>(q, kc, vc, o);
  k_wo<<<D / 64, 256, 0, stream>>>(o, wo, x, mod, x1);
  k_w1<<<F / 64, 256, 0, stream>>>(x1, mod, w1, b1, h1);
  k_w2<<<D / 64, 256, 0, stream>>>(h1, w2, b2, x1, mod, (u16*)d_out);
}
```

```cpp
#define AUX_W 0
#include <hip/hip_runtime.h>
#include <hip/hip_bf16.h>
#include <stdint.h>

typedef unsigned short u16;
typedef unsigned u32;
typedef unsigned long long u64;
typedef u32 v4u __attribute__((ext_vector_type(4)));
typedef u32 v2u __attribute__((ext_vector_type(2)));
typedef float f32x4 __attribute__((ext_vector_type(4)));
typedef float f32x2 __attribute__((ext_vector_type(2)));
typedef __bf16 bf16x8 __attribute__((ext_vector_type(8)));
typedef __bf16 bf16x2 __attribute__((ext_vector_type(2)));
typedef short s16x4 __attribute__((ext_vector_type(4)));
typedef __amdgpu_buffer_rsrc_t rsrc_t;
#define LAS __attribute__((address_space(3)))
#define DEV __device__ __forceinline__

#define KV_WIN 8
#define AUX_KV 2
#define D_DELAY 0
#define A2_DELAY 0
#define Q_DELAY 40

constexpr int D = 512, SK = 8192, WIN = 2048, FF = 1792;
constexpr int AUX_SC1 = 16;
constexpr u32 SENT = 0xFFFFFFFFu;
constexpr u32 SPIN_CAP = 1u << 16;
constexpr u32 OOB = 0x7FFFFF00u;

constexpr u32 WS_EPOCH = 0;
constexpr u32 WS_FLAG = 4096;
constexpr u32 WS_H = 65536;
constexpr u32 WS_Q = WS_H + 262144;
constexpr u32 WS_O = WS_Q + 65536;
constexpr u32 WS_LSE = WS_O + 131072;
constexpr u32 WS_Y = WS_LSE + 16384;
constexpr u32 WS_M2 = WS_Y + 131072;
constexpr u32 WS_P = WS_M2 + 32768;
constexpr u32 WS_END = WS_P + 262144;
constexpr u32 WS_STAMP = 1u << 20;

constexpr int L_K = 0;
constexpr int L_RED = 65536;
constexpr int L_QS = L_RED + 8192;
constexpr int L_ST = L_QS + 1536;
constexpr int L_END = L_ST + 512;

struct Args {
  const u16 *kc, *vc; unsigned char* ws; const u16 *wq, *wmod, *x, *c, *wo, *w1, *w2, *b1, *b2; u16* out;
};

#define STAMP(i)
#define STAMP_ARG
#define STAMP_PASS

DEV rsrc_t mkrs(const void* p, u32 bytes) { return __builtin_amdgcn_make_buffer_rsrc((void*)p, 0, (int)bytes, 0x00020000); }
template <int AUX> DEV v4u ld128(rsrc_t r, u32 vo, u32 so) { return __builtin_amdgcn_raw_buffer_load_b128(r, vo, so, AUX); }
template <int AUX> DEV v2u ld64(rsrc_t r, u32 vo, u32 so) { return __builtin_amdgcn_raw_buffer_load_b64(r, vo, so, AUX); }
template <int AUX> DEV u32 ld32(rsrc_t r, u32 vo, u32 so) { return __builtin_amdgcn_raw_buffer_load_b32(r, vo, so, AUX); }
DEV void st128(rsrc_t r, u32 vo, v4u v) { __builtin_amdgcn_raw_buffer_store_b128(v, r, vo, 0, AUX_SC1); }
DEV void st64(rsrc_t r, u32 vo, v2u v) { __builtin_amdgcn_raw_buffer_store_b64(v, r, vo, 0, AUX_SC1); }
DEV void st32(rsrc_t r, u32 vo, u32 v) { __builtin_amdgcn_raw_buffer_store_b32(v, r, vo, 0, AUX_SC1); }
DEV void cfence() { asm volatile("" ::: "memory"); }
DEV void sched0() { __builtin_amdgcn_sched_barrier(0); }
DEV void wg_barrier() { asm volatile("s_waitcnt lgkmcnt(0)\n\ts_barrier" ::: "memory"); }
DEV void lds_wait() { asm volatile("s_waitcnt lgkmcnt(0)" ::: "memory"); }
DEV void nap(int n) { for (int i = 0; i < n; ++i) __builtin_amdgcn_s_sleep(1); }

DEV float asf(u32 u) { return __builtin_bit_cast(float, u); }
DEV u32 asu(float f) { return __builtin_bit_cast(u32, f); }
DEV float bflo(u32 u) { return asf(u << 16); }
DEV float bfhi(u32 u) { return asf(u & 0xFFFF0000u); }
DEV u32 pk2(float lo, float hi) { f32x2 v = {lo, hi}; return __builtin_bit_cast(u32, __builtin_convertvector(v, bf16x2)); }
DEV u32 unsent(u32 v) { return v < SENT ? v : SENT - 1u; }
DEV u32 umax3(u32 a, u32 b, u32 c) { u32 m = a > b ? a : b; return m > c ? m : c; }
DEV u32 umax4(v4u v, u32 acc) { return umax3(v.x, v.y, umax3(v.z, v.w, acc)); }
DEV u32 perm_lo(u32 r1, u32 r0) { return __builtin_amdgcn_perm(r1, r0, 0x05040100u); }
DEV u32 perm_hi(u32 r1, u32 r0) { return __builtin_amdgcn_perm(r1, r0, 0x07060302u); }
template <int CTRL> DEV float dpp(float x) { return asf((u32)__builtin_amdgcn_mov_dpp((int)asu(x), CTRL, 0xf, 0xf, true)); }
constexpr int X1 = 0xB1, X2 = 0x4E, ROR4 = 0x124, ROR8 = 0x128;
DEV float swap16_add(float a, float b) { auto s = __builtin_amdgcn_permlane16_swap(asu(a), asu(b), false, false); return asf(s[0]) + asf(s[1]); }
DEV float swap32_add(float a, float b) { auto s = __builtin_amdgcn_permlane32_swap(asu(a), asu(b), false, false); return asf(s[0]) + asf(s[1]); }
DEV float swap16_max(float a) { auto s = __builtin_amdgcn_permlane16_swap(asu(a), asu(a), false, false); return fmaxf(asf(s[0]), asf(s[1])); }
DEV float swap32_max(float a) { auto s = __builtin_amdgcn_permlane32_swap(asu(a), asu(a), false, false); return fmaxf(asf(s[0]), asf(s[1])); }
DEV float parity_sum(float v) { v += dpp<X2>(v); v += dpp<ROR4>(v); v += dpp<ROR8>(v); v = swap16_add(v, v); return swap32_add(v, v); }
DEV f32x4 mfma32(v4u a, v4u b, f32x4 c) { return __builtin_amdgcn_mfma_f32_16x16x32_bf16(__builtin_bit_cast(bf16x8, a), __builtin_bit_cast(bf16x8, b), c, 0, 0, 0); }
DEV f32x4 mfma16(v2u a, v2u b, f32x4 c) { return __builtin_amdgcn_mfma_f32_16x16x16bf16_1k(__builtin_bit_cast(s16x4, a), __builtin_bit_cast(s16x4, b), c, 0, 0, 0); }
DEV float silu(float v) { return v * __builtin_amdgcn_rcpf(1.f + __builtin_amdgcn_exp2f(-1.4426950408889634f * v)); }
DEV bool all_true(bool ok) { return __builtin_amdgcn_ballot_w64(!ok) == 0; }

DEV void tr8x8(const v4u (&r)[8], v4u (&f)[8]) {
#pragma unroll
  for (int jj = 0; jj < 4; ++jj)
#pragma unroll
    for (int d = 0; d < 4; ++d) { f[2 * d][jj] = perm_lo(r[2 * jj + 1][d], r[2 * jj][d]); f[2 * d + 1][jj] = perm_hi(r[2 * jj + 1][d], r[2 * jj][d]); }
}

DEV void wait_flags(rsrc_t ws, u32 off, v2u tag) {
  for (u32 sp = 0; sp < SPIN_CAP; ++sp) {
    cfence();
    const v2u f = ld64<AUX_SC1>(ws, off, 0);
    if (all_true(f.x == tag.x && f.y == tag.y)) break;
    __builtin_amdgcn_s_sleep(4);
  }
  cfence();
}
DEV v2u next_tag(v2u e) { v2u t; t.x = e.x + 1u; t.y = e.y + (t.x == 0u ? 1u : 0u); return t; }
DEV u32 flag_off(int g, int r) { return WS_FLAG + (u32)(r * 8 + g) * 128u; }

DEV void role_attn(STAMP_ARG const Args& a, LAS unsigned char* L, int g, int r, int w, int l) {
  const int b = r >> 3, s = r & 7;
  const rsrc_t ws = mkrs(a.ws, WS_END);
  const int c = l & 15, gq = l >> 4;
  if (w == 4) {
    const v2u ep = ld64<AUX_SC1>(ws, WS_EPOCH, 0);
    const v4u sv = {SENT, SENT, SENT, SENT};
    st128(ws, WS_O + (u32)(((g * 4 + gq) * 16 + r) * 256 + c * 16), sv);
    st128(ws, l < 8 ? WS_LSE + (u32)((g * 16 + r) * 128 + l * 16) : OOB, sv);
    asm volatile("s_waitcnt vmcnt(0)" ::: "memory");
    const v2u tag = next_tag(ep);
    st64(ws, l == 0 ? flag_off(g, r) : OOB, tag);
    if (l == 0) *(LAS v2u*)(L + L_ST + 256) = tag;
    STAMP(1);
    nap(Q_DELAY);
    wait_flags(ws, flag_off(g, 16 + (l & 7)), tag);
    wait_flags(ws, flag_off(l & 7, 24 + (l >> 3)), tag);
    STAMP(2);
    v4u q[4];
    const u32 qo = WS_Q + (u32)((g * 2 + b) * 4096 + l * 16);
    for (u32 sp = 0; sp < SPIN_CAP; ++sp) {
      cfence();
      u32 mx = 0;
#pragma unroll
      for (int k = 0; k < 4; ++k) q[k] = ld128<AUX_SC1>(ws, qo + k * 1024, 0);
#pragma unroll
      for (int k = 0; k < 4; ++k) mx = umax4(q[k], mx);
      if (all_true(mx != SENT)) break;
    }
    STAMP(3);
    constexpr float QS = 0.08838834764831845f * 1.4426950408889634f;
    v4u o;
#pragma unroll
    for (int d = 0; d < 4; ++d) {
      const float lo = (bflo(q[0][d]) + bflo(q[1][d])) + (bflo(q[2][d]) + bflo(q[3][d]));
      const float hi = (bfhi(q[0][d]) + bfhi(q[1][d])) + (bfhi(q[2][d]) + bfhi(q[3][d]));
      o[d] = pk2(lo * QS, hi * QS);
    }
    *(LAS v4u*)(L + L_QS + gq * 320 + c * 16) = o;
    wg_barrier();
    return;
  }
  const u32 kvbase = (u32)(((b * 8 + g) * SK + (SK - WIN) + s * 256 + w * 64) * 256);
  constexpr u32 KVBYTES = 2u * 8u * SK * 256u;
  {
    const u64 kp = (u64)a.kc;
    const v4u srd = {(u32)kp, (u32)(kp >> 32) & 0xFFFFu, KVBYTES, 0x00020000u};
    u32 vo[4];
#pragma unroll
    for (int i = 0; i < 4; ++i) vo[i] = (u32)(gq * 256 + ((c ^ (4 * i + gq)) * 16));
    const u32 lbase = (u32)(size_t)(L + L_K) + (u32)w * 16384u;
#pragma unroll
    for (int u = 0; u < 4; ++u) {
      const u32 m0v = lbase + u * 4096u, so = kvbase + u * 4096u;
#define KPOL " nt"
#define KLOAD(i)                                                                                                    \
  if (4 * u + i >= KV_WIN) asm volatile("s_waitcnt vmcnt(%0)" ::"n"(KV_WIN - 1) : "memory");                        \
  asm volatile("s_mov_b32 m0, %0\n\ts_nop 0\n\tbuffer_load_dwordx4 %1, %2, %3 offen offset:%4" KPOL " lds" ::"s"(m0v), \
               "v"(vo[i]), "s"(srd), "s"(so), "n"(i * 1024) : "memory");
      KLOAD(0) KLOAD(1) KLOAD(2) KLOAD(3)
#undef KLOAD
    }
  }
  const rsrc_t vr = mkrs(a.vc, KVBYTES);
  v4u vv[2][8];
#pragma unroll
  for (int t = 0; t < 2; ++t)
#pragma unroll
    for (int j = 0; j < 8; ++j) {
      if (16 + 8 * t + j >= KV_WIN) asm volatile("s_waitcnt vmcnt(%0)" ::"n"(KV_WIN - 1) : "memory");
      vv[t][j] = ld128<AUX_KV>(vr, (u32)(gq * 1024 + c * 16 + (j & 3) * 256), kvbase + (u32)(2 * t + (j >> 2)) * 4096u);
      sched0();
    }
  asm volatile("s_waitcnt vmcnt(16)" ::: "memory");
  STAMP(1);
  v4u kf[4][4];
#pragma unroll
  for (int u = 0; u < 4; ++u)
#pragma unroll
    for (int k = 0; k < 4; ++k) kf[u][k] = *(const LAS v4u*)(L + L_K + w * 16384 + u * 4096 + c * 256 + (((4 * k + gq) ^ c) * 16));
  wg_barrier();
  STAMP(2);
  v4u qf[4];
#pragma unroll
  for (int k = 0; k < 4; ++k) qf[k] = *(const LAS v4u*)(L + L_QS + (c >> 2) * 320 + (4 * k + gq) * 16);
  f32x4 sc[4];
#pragma unroll
  for (int u = 0; u < 4; ++u) {
    sc[u] = f32x4{0.f, 0.f, 0.f, 0.f};
#pragma unroll
    for (int k = 0; k < 4; ++k) sc[u] = mfma32(kf[u][k], qf[k], sc[u]);
  }
  float m = -INFINITY;
#pragma unroll
  for (int u = 0; u < 4; ++u) m = fmaxf(fmaxf(m, fmaxf(sc[u][0], sc[u][1])), fmaxf(sc[u][2], sc[u][3]));
  m = swap32_max(swap16_max(m));
  float ls = 0.f;
#pragma unroll
  for (int u = 0; u < 4; ++u)
#pragma unroll
    for (int e = 0; e < 4; ++e) { sc[u][e] = __builtin_amdgcn_exp2f(sc[u][e] - m); ls += sc[u][e]; }
  ls = swap16_add(ls, ls); ls = swap32_add(ls, ls);
  LAS float* st = (LAS float*)(L + L_ST);
  if (l < 16 && (l & 3) == 0) { st[(l >> 2) * 8 + w] = m; st[(l >> 2) * 8 + 4 + w] = ls; }
  wg_barrier();
  const f32x4 mw = *(const LAS f32x4*)(st + (c >> 2) * 8), lw = *(const LAS f32x4*)(st + (c >> 2) * 8 + 4);
  const float M = fmaxf(fmaxf(mw[0], mw[1]), fmaxf(mw[2], mw[3]));
  const float Lt = (lw[0] * __builtin_amdgcn_exp2f(mw[0] - M) + lw[1] * __builtin_amdgcn_exp2f(mw[1] - M)) +
                   (lw[2] * __builtin_amdgcn_exp2f(mw[2] - M) + lw[3] * __builtin_amdgcn_exp2f(mw[3] - M));
  const float fs = __builtin_amdgcn_exp2f(m - M) * __builtin_amdgcn_rcpf(Lt);
  if (w == 0) st32(ws, (l < 16 && (l & 3) == 0) ? WS_LSE + (u32)((g * 16 + r) * 128 + l) : OOB, unsent(asu(M + __builtin_amdgcn_logf(Lt))));
  v4u pf[2];
#pragma unroll
  for (int t = 0; t < 2; ++t) {
    pf[t][0] = pk2(sc[2 * t][0] * fs, sc[2 * t][1] * fs); pf[t][1] = pk2(sc[2 * t][2] * fs, sc[2 * t][3] * fs);
    pf[t][2] = pk2(sc[2 * t + 1][0] * fs, sc[2 * t + 1][1] * fs); pf[t][3] = pk2(sc[2 * t + 1][2] * fs, sc[2 * t + 1][3] * fs);
  }
  STAMP(3);
  f32x4 oa[8];
#pragma unroll
  for (int i = 0; i < 8; ++i) oa[i] = f32x4{0.f, 0.f, 0.f, 0.f};
#pragma unroll
  for (int t = 0; t < 2; ++t) {
    v4u vf[8];
    tr8x8(vv[t], vf);
#pragma unroll
    for (int i = 0; i < 8; ++i) oa[i] = mfma32(pf[t], vf[i], oa[i]);
  }
  *(LAS f32x4*)(L + L_RED + (w * 2) * 1024 + l * 16) = f32x4{oa[0][0], oa[1][0], oa[2][0], oa[3][0]};
  *(LAS f32x4*)(L + L_RED + (w * 2 + 1) * 1024 + l * 16) = f32x4{oa[4][0], oa[5][0], oa[6][0], oa[7][0]};
  STAMP(4);
  wg_barrier();
  {
    f32x2 acc = {0.f, 0.f};
#pragma unroll
    for (int ww = 0; ww < 4; ++ww) acc += *(const LAS f32x2*)(L + L_RED + (ww * 2 + ((l >> 1) & 1)) * 1024 + (w * 16 + (l >> 2)) * 16 + (l & 1) * 8);
    st32(ws, WS_O + (u32)(((g * 4 + w) * 16 + r) * 256 + l * 4), unsent(pk2(acc[0], acc[1])));
  }
  STAMP(5);
  if (w != 0) return;
  const int line = g * 4 + (r >> 2), oct = r & 3, hf = l & 1, p = l >> 1;
  const u32 po = WS_P + (u32)(line * 8192 + p * 128 + oct * 32 + hf * 16);
  v4u p0, p1;
  for (u32 sp = 0; sp < SPIN_CAP; ++sp) {
    cfence();
    p0 = ld128<AUX_SC1>(ws, po, 0);
    p1 = ld128<AUX_SC1>(ws, p < 24 ? po + 4096 : OOB, 0);
    if (all_true(umax4(p0, umax4(p1, 0)) != SENT)) break;
  }
  STAMP(6);
  const float u0 = swap32_add(asf(p0[0]) + asf(p1[0]), asf(p0[2]) + asf(p1[2]));
  const float u1 = swap32_add(asf(p0[1]) + asf(p1[1]), asf(p0[3]) + asf(p1[3]));
  float v = swap16_add(u0, u1);
  v += dpp<ROR8>(v); v += dpp<ROR4>(v); v += dpp<X2>(v);
  if ((l & 14) == 0) {
    const int col = (line & 15) * 32 + (2 * (oct & 1) + hf) * 8 + (oct >> 1) * 4 + gq;
    a.out[(line >> 4) * D + col] = (u16)pk2(v, v);
  }
  if (g == 0 && r == 0) st64(ws, l == 0 ? WS_EPOCH : OOB, *(const LAS v2u*)(L + L_ST + 256));
  STAMP(7);
}

DEV void b_load(const Args& a, int g, int j, int w, int l, v4u (&wr)[8]) {
  const rsrc_t rs = mkrs(a.wq, 512u * 4096u * 2u);
  const int kq = j & 3, hh = j >> 2;
  const u32 vo = (u32)((l >> 4) * 8 * 8192 + (l & 15) * 16), so = (u32)((128 * kq + 32 * w) * 8192 + (g * 4 + hh) * 256);
#pragma unroll
  for (int jj = 0; jj < 8; ++jj) wr[jj] = ld128<AUX_W>(rs, vo, so + jj * 8192u);
}
DEV void b_run(STAMP_ARG rsrc_t ws, LAS unsigned char* L, int g, int j, int w, int l, const v4u (&wr)[8], u32 tag16) {
  const int kq = j & 3, hh = j >> 2, gq = l >> 4;
  v4u wf[8];
  tr8x8(wr, wf);
  STAMP(3);
  const u32 ho = WS_H + (u32)(g * 32768 + (kq * 4 + w) * 2048 + ((l & 15) >> 1) * 256 + gq * 64 + (l & 1) * 16);
  v4u h0, h1;
  for (u32 sp = 0; sp < SPIN_CAP; ++sp) {
    cfence();
    h0 = ld128<AUX_SC1>(ws, ho, 0);
    h1 = ld128<AUX_SC1>(ws, ho + 32, 0);
    u32 bad = 0;
#pragma unroll
    for (int d = 0; d < 4; ++d) bad |= (h0[d] ^ tag16) | (h1[d] ^ tag16);
    if (all_true((bad & 0xFFFFu) == 0)) break;
  }
  STAMP(4);
  const v4u af = {perm_hi(h0[1], h0[0]), perm_hi(h0[3], h0[2]), perm_hi(h1[1], h1[0]), perm_hi(h1[3], h1[2])};
  float t[8];
#pragma unroll
  for (int i = 0; i < 8; ++i) {
    const f32x4 d = mfma32(af, wf[i], f32x4{0.f, 0.f, 0.f, 0.f});
    t[i] = swap32_add(d[0] + d[2], d[1] + d[3]);
  }
  const f32x4 o = {swap16_add(t[0], t[4]), swap16_add(t[1], t[5]), swap16_add(t[2], t[6]), swap16_add(t[3], t[7])};
  *(LAS f32x4*)(L + L_RED + 4096 + w * 1024 + (gq >> 1) * 512 + (l & 15) * 32 + (gq & 1) * 16) = o;
  wg_barrier();
  if (w < 2) {
    f32x2 acc = {0.f, 0.f};
#pragma unroll
    for (int ww = 0; ww < 4; ++ww) acc += *(const LAS f32x2*)(L + L_RED + 4096 + ww * 1024 + w * 512 + l * 8);
    st32(ws, WS_Q + (u32)((g * 2 + w) * 4096 + kq * 1024 + hh * 256 + l * 4), unsent(pk2(acc[0], acc[1])));
  }
  STAMP(5);
}
DEV void clear_q(rsrc_t ws, int g, int j, int l, v4u sv) {
  st128(ws, l < 32 ? WS_Q + (u32)((g * 2 + (l >> 4)) * 4096 + (j & 3) * 1024 + (j >> 2) * 256 + (l & 15) * 16) : OOB, sv);
}

DEV void role_f0(STAMP_ARG const Args& a, LAS unsigned char* L, int g, int i0, int w, int l) {
  const rsrc_t ws = mkrs(a.ws, WS_END);
  const int c = l & 15, gq = l >> 4;
  if (w == 4) {
    const v2u ep = ld64<AUX_SC1>(ws, WS_EPOCH, 0);
    const v4u sv = {SENT, SENT, SENT, SENT};
    clear_q(ws, g, i0, l, sv);
#pragma unroll
    for (int k = 0; k < 2; ++k) st128(ws, WS_Y + (u32)((4 * k + gq) * 16384 + (i0 * 8 + g) * 256 + c * 16), sv);
    wg_barrier();
    asm volatile("s_waitcnt vmcnt(0)" ::: "memory");
    const v2u tag = next_tag(ep);
    st64(ws, l == 0 ? flag_off(g, 16 + i0) : OOB, tag);
    STAMP(1);
    wg_barrier();
    wait_flags(ws, flag_off(g, c), tag);
    STAMP(2);
    wg_barrier();
    return;
  }
  const rsrc_t wm = mkrs(a.wmod, 512u * 3072u * 2u), xr = mkrs(a.x, 2048), cr = mkrs(a.c, 2048);
  const int p = i0, dg = g, pb = w >> 1, ph = w & 1;
  v4u ar[4];
  const v2u cv = ld64<0>(cr, (u32)(((c >> 3) * 512 + 64 * p + 16 * w + 4 * gq) * 2), 0);
#pragma unroll
  for (int jj = 0; jj < 4; ++jj)
    ar[jj] = ld128<AUX_W>(wm, (u32)(gq * 4 * 6144 + (c >> 3) * 1024 + (c & 7) * 16), (u32)((64 * p + 16 * w + jj) * 6144 + dg * 128));
  const int hb = l & 1, hcol = 16 * w + 4 * ((l >> 1) & 3);
  const v4u xv0 = ld128<0>(xr, (u32)(hb * 1024 + (l >> 1) * 16), 0), xv1 = ld128<0>(xr, (u32)(hb * 1024 + 512 + (l >> 1) * 16), 0);
  const v2u x4 = ld64<0>(xr, (u32)(hb * 1024 + (64 * dg + hcol) * 2), 0);
  const u32 ep = ld32<AUX_SC1>(ws, WS_EPOCH, 0);
  v4u wr[8];
  b_load(a, g, i0, w, l, wr);
  sched0();
  const v2u sa = {pk2(silu(bflo(cv.x)), silu(bfhi(cv.x))), pk2(silu(bflo(cv.y)), silu(bfhi(cv.y)))};
  float d0[8];
#pragma unroll
  for (int d = 0; d < 4; ++d) {
    const v2u flo = {perm_lo(ar[1][d], ar[0][d]), perm_lo(ar[3][d], ar[2][d])}, fhi = {perm_hi(ar[1][d], ar[0][d]), perm_hi(ar[3][d], ar[2][d])};
    d0[2 * d] = mfma16(sa, flo, f32x4{0.f, 0.f, 0.f, 0.f})[0];
    d0[2 * d + 1] = mfma16(sa, fhi, f32x4{0.f, 0.f, 0.f, 0.f})[0];
  }
  STAMP(1);
  {
    const bool up = gq & 1;
    const f32x4 o = {up ? d0[4] : d0[0], up ? d0[5] : d0[1], up ? d0[6] : d0[2], up ? d0[7] : d0[3]};
    *(LAS f32x4*)(L + L_RED + w * 1024 + (gq >> 1) * 512 + c * 32 + (gq & 1) * 16) = o;
  }
  float xs[16], xsum = 0.f, vs = 0.f;
#pragma unroll
  for (int d = 0; d < 4; ++d) { xs[2 * d] = bflo(xv0[d]); xs[2 * d + 1] = bfhi(xv0[d]); xs[8 + 2 * d] = bflo(xv1[d]); xs[9 + 2 * d] = bfhi(xv1[d]); }
#pragma unroll
  for (int d = 0; d < 16; ++d) xsum += xs[d];
  const float mean = parity_sum(xsum) * (1.f / D);
#pragma unroll
  for (int d = 0; d < 16; ++d) vs += (xs[d] - mean) * (xs[d] - mean);
  const float rstd = __builtin_amdgcn_rsqf(parity_sum(vs) * (1.f / D) + 1e-5f);
  const f32x4 nn = {(bflo(x4.x) - mean) * rstd, (bfhi(x4.x) - mean) * rstd, (bflo(x4.y) - mean) * rstd, (bfhi(x4.y) - mean) * rstd};
  const u32 tag16 = (ep + 1u) & 0xFFFFu;
  wg_barrier();
  {
    f32x4 s4 = {0.f, 0.f, 0.f, 0.f}, c4 = {0.f, 0.f, 0.f, 0.f};
#pragma unroll
    for (int ww = 0; ww < 4; ++ww) {
      s4 += *(const LAS f32x4*)(L + L_RED + ww * 1024 + hb * 512 + hcol * 4);
      c4 += *(const LAS f32x4*)(L + L_RED + ww * 1024 + hb * 512 + 256 + hcol * 4);
    }
    const float one = p == 0 ? 1.f : 0.f;
    const u32 h01 = pk2(nn[0] * (c4[0] + one) + s4[0], nn[1] * (c4[1] + one) + s4[1]);
    const u32 h23 = pk2(nn[2] * (c4[2] + one) + s4[2], nn[3] * (c4[3] + one) + s4[3]);
    const v4u hv = {(h01 << 16) | tag16, (h01 & 0xFFFF0000u) | tag16, (h23 << 16) | tag16, (h23 & 0xFFFF0000u) | tag16};
    st128(ws, WS_H + (u32)((l >> 3) * 32768 + (dg * 2 + (w >> 1)) * 2048 + p * 256 + (w & 1) * 128 + (l & 7) * 16), hv);
  }
  STAMP(2);
  b_run(STAMP_PASS ws, L, g, i0, w, l, wr, tag16);
  nap(D_DELAY);
  const int cg = i0;
  const rsrc_t wo = mkrs(a.wo, 4096u * 512u * 2u);
  v2u orow[4][8];
#pragma unroll
  for (int k = 0; k < 4; ++k)
#pragma unroll
    for (int jj = 0; jj < 8; ++jj)
      orow[k][jj] = ld64<AUX_W>(wo, (u32)(gq * 32 * 1024 + c * 8 + (jj & 3) * 1024), (u32)(((g * 4 + w) * 128 + 8 * k + (jj & 4)) * 1024 + cg * 128));
  sched0();
  v4u of[4][4];
#pragma unroll
  for (int k = 0; k < 4; ++k)
#pragma unroll
    for (int jj = 0; jj < 4; ++jj)
#pragma unroll
      for (int d = 0; d < 2; ++d) { of[k][2 * d][jj] = perm_lo(orow[k][2 * jj + 1][d], orow[k][2 * jj][d]); of[k][2 * d + 1][jj] = perm_hi(orow[k][2 * jj + 1][d], orow[k][2 * jj][d]); }
  STAMP(6);
  wg_barrier();
  const int sg = (l >> 3) & 1, ob = (l >> 2) & 1, kp = l & 3;
  float wt[4];
  {
    u32 ls[4];
    const u32 lo = WS_LSE + (u32)((g * 16 + ob * 8 + sg * 4) * 128 + w * 4);
    for (u32 sp = 0; sp < SPIN_CAP; ++sp) {
      cfence();
#pragma unroll
      for (int i = 0; i < 4; ++i) ls[i] = ld32<AUX_SC1>(ws, lo + i * 128, 0);
      if (all_true(umax3(ls[0], ls[1], umax3(ls[2], ls[3], 0)) != SENT)) break;
    }
    float M = fmaxf(fmaxf(asf(ls[0]), asf(ls[1])), fmaxf(asf(ls[2]), asf(ls[3])));
    M = fmaxf(M, dpp<ROR8>(M));
    float Ls = 0.f;
#pragma unroll
    for (int i = 0; i < 4; ++i) { wt[i] = __builtin_amdgcn_exp2f(asf(ls[i]) - M); Ls += wt[i]; }
    Ls += dpp<ROR8>(Ls);
    const float inv = __builtin_amdgcn_rcpf(Ls);
#pragma unroll
    for (int i = 0; i < 4; ++i) wt[i] *= inv;
  }
  STAMP(7);
  v4u ov[4];
  const u32 oo = WS_O + (u32)(((g * 4 + w) * 16 + ob * 8 + sg * 4) * 256 + gq * 64 + kp * 16);
  for (u32 sp = 0; sp < SPIN_CAP; ++sp) {
    cfence();
    u32 mx = 0;
#pragma unroll
    for (int i = 0; i < 4; ++i) ov[i] = ld128<AUX_SC1>(ws, oo + i * 256, 0);
#pragma unroll
    for (int i = 0; i < 4; ++i) mx = umax4(ov[i], mx);
    if (all_true(mx != SENT)) break;
  }
  STAMP(8);
  v4u af;
#pragma unroll
  for (int d = 0; d < 4; ++d) {
    const float lo = (wt[0] * bflo(ov[0][d]) + wt[1] * bflo(ov[1][d])) + (wt[2] * bflo(ov[2][d]) + wt[3] * bflo(ov[3][d]));
    const float hi = (wt[0] * bfhi(ov[0][d]) + wt[1] * bfhi(ov[1][d])) + (wt[2] * bfhi(ov[2][d]) + wt[3] * bfhi(ov[3][d]));
    af[d] = pk2(lo, hi);
  }
  float t[4];
#pragma unroll
  for (int i = 0; i < 4; ++i) {
    float e[4];
#pragma unroll
    for (int k = 0; k < 4; ++k) e[k] = mfma32(af, of[k][i], f32x4{0.f, 0.f, 0.f, 0.f})[k];
    t[i] = (e[0] + e[1]) + (e[2] + e[3]);
  }
  {
    const f32x2 o = {swap32_add(t[0], t[2]), swap32_add(t[1], t[3])};
    *(LAS f32x2*)(L + L_RED + ((w * 2 + (gq & 1)) * 64 + 4 * c + 2 * (gq >> 1)) * 4) = o;
  }
  wg_barrier();
  {
    f32x4 acc = {0.f, 0.f, 0.f, 0.f};
#pragma unroll
    for (int ww = 0; ww < 4; ++ww) acc += *(const LAS f32x4*)(L + L_RED + ((ww * 2 + pb) * 64 + 4 * c) * 4);
    const v2u yv = {unsent(pk2(acc[0], acc[1])), unsent(pk2(acc[2], acc[3]))};
    st64(ws, WS_Y + (u32)((4 * ph + gq) * 16384 + (cg * 8 + g) * 256 + pb * 128 + c * 8), yv);
  }
  STAMP(9);
}

DEV void role_f1(STAMP_ARG const Args& a, LAS unsigned char* L, int g, int i0, int w, int l) {
  const rsrc_t ws = mkrs(a.ws, WS_END);
  const int c = l & 15, gq = l >> 4;
  const int cgp = g * 4 + (i0 >> 1), part = i0 & 1, e = g * 8 + i0;
  const u32 m2o = WS_M2 + (u32)(((cgp >> 3) * 2 + part) * 4096 + (cgp & 7) * 256);
  if (w == 4) {
    const v2u ep = ld64<AUX_SC1>(ws, WS_EPOCH, 0);
    const v4u sv = {SENT, SENT, SENT, SENT};
    clear_q(ws, g, 8 + i0, l, sv);
    st128(ws, l < 32 ? m2o + (u32)(gq * 2048 + c * 16) : OOB, sv);
#pragma unroll
    for (int k = 0; k < 4; ++k) st128(ws, WS_P + (u32)((8 * k + (l >> 3)) * 8192 + e * 128 + (l & 7) * 16), sv);
    asm volatile("s_waitcnt vmcnt(0)" ::: "memory");
    const v2u tag = next_tag(ep);
    st64(ws, l == 0 ? flag_off(g, 24 + i0) : OOB, tag);
    STAMP(1);
    wg_barrier();
    wait_flags(ws, flag_off(l & 7, 16 + (l >> 3)), tag);
    wait_flags(ws, flag_off(l & 7, 24 + (l >> 3)), tag);
    STAMP(2);
    wg_barrier();
    return;
  }
  const u32 ep = ld32<AUX_SC1>(ws, WS_EPOCH, 0);
  v4u wr[8];
  b_load(a, g, 8 + i0, w, l, wr);
  sched0();
  b_run(STAMP_PASS ws, L, g, 8 + i0, w, l, wr, (ep + 1u) & 0xFFFFu);
  nap(A2_DELAY);
  {
    const rsrc_t wm = mkrs(a.wmod, 512u * 3072u * 2u), cr = mkrs(a.c, 2048);
    v4u cv[2];
    v2u mr[2][8];
#pragma unroll
    for (int k = 0; k < 2; ++k) {
      cv[k] = ld128<0>(cr, (u32)(((c >> 3) * 512 + 256 * part + 64 * w + 32 * k + 8 * gq) * 2), 0);
#pragma unroll
      for (int jj = 0; jj < 8; ++jj)
        mr[k][jj] = ld64<AUX_W>(wm, (u32)(gq * 8 * 6144 + c * 8), (u32)((256 * part + 64 * w + 32 * k + jj) * 6144 + 2048 + cgp * 128));
    }
    sched0();
    f32x4 acc[4];
#pragma unroll
    for (int i = 0; i < 4; ++i) acc[i] = f32x4{0.f, 0.f, 0.f, 0.f};
#pragma unroll
    for (int k = 0; k < 2; ++k) {
      v4u sa, f[4];
#pragma unroll
      for (int d = 0; d < 4; ++d) sa[d] = pk2(silu(bflo(cv[k][d])), silu(bfhi(cv[k][d])));
#pragma unroll
      for (int jj = 0; jj < 4; ++jj)
#pragma unroll
        for (int d = 0; d < 2; ++d) { f[2 * d][jj] = perm_lo(mr[k][2 * jj + 1][d], mr[k][2 * jj][d]); f[2 * d + 1][jj] = perm_hi(mr[k][2 * jj + 1][d], mr[k][2 * jj][d]); }
#pragma unroll
      for (int i = 0; i < 4; ++i) acc[i] = mfma32(sa, f[i], acc[i]);
    }
    if ((gq & 1) == 0) *(LAS f32x4*)(L + L_RED + w * 512 + (gq >> 1) * 256 + c * 16) = f32x4{acc[0][0], acc[1][0], acc[2][0], acc[3][0]};
    wg_barrier();
    if (w < 2) {
      float sv = 0.f;
#pragma unroll
      for (int ww = 0; ww < 4; ++ww) sv += *(const LAS float*)(L + L_RED + ww * 512 + w * 256 + l * 4);
      st32(ws, m2o + (u32)(w * 2048 + l * 4), unsent(asu(sv)));
    }
  }
  STAMP(6);
  if (g == 7) return;
  const rsrc_t w1 = mkrs(a.w1, 512u * FF * 2u), w2 = mkrs(a.w2, FF * 512u * 2u), xr = mkrs(a.x, 2048), b1r = mkrs(a.b1, FF * 2), b2r = mkrs(a.b2, 1024);
  const int rho = (l >> 3) & 1, eb = (l >> 2) & 1, kp = l & 3;
  const int d0 = 128 * w + 32 * gq + 8 * kp;
  const int fb = gq >> 1, fcol = 128 * w + 8 * c + 4 * (gq & 1);
  u32 r1[4][8];
  v4u r2[8];
#pragma unroll
  for (int k = 0; k < 4; ++k)
#pragma unroll
    for (int jj = 0; jj < 8; ++jj) r1[k][jj] = ld32<AUX_W>(w1, (u32)(gq * 32 * 3584 + c * 4), (u32)((128 * w + 8 * k + jj) * 3584 + e * 64));
#pragma unroll
  for (int jj = 0; jj < 8; ++jj) r2[jj] = ld128<AUX_W>(w2, (u32)(gq * 8192 + c * 16 + (jj & 3) * 1024), (u32)((32 * e + (jj & 4)) * 1024 + w * 256));
  const v4u xv = ld128<0>(xr, (u32)((eb * 512 + d0) * 2), 0);
  const u32 b1v = __builtin_amdgcn_raw_buffer_load_b16(b1r, (u32)((32 * e + (l & 31)) * 2), 0, 0);
  const v2u b2v = ld64<0>(b2r, (u32)(fcol * 2), 0);
  sched0();
  v4u f1[4][2], f2[8];
#pragma unroll
  for (int k = 0; k < 4; ++k)
#pragma unroll
    for (int jj = 0; jj < 4; ++jj) { f1[k][0][jj] = perm_lo(r1[k][2 * jj + 1], r1[k][2 * jj]); f1[k][1][jj] = perm_hi(r1[k][2 * jj + 1], r1[k][2 * jj]); }
  tr8x8(r2, f2);
  float ga[8], sb[8], cb[8], gb[4];
  {
    v4u m[8];
    const u32 mo = WS_M2 + (u32)(rho * 4096 + eb * 2048 + d0 * 4), go = WS_M2 + (u32)(3 * 8192 + fb * 2048 + fcol * 4);
    for (u32 sp = 0; sp < SPIN_CAP; ++sp) {
      cfence();
      u32 mx = 0;
#pragma unroll
      for (int q = 0; q < 3; ++q) { m[2 * q] = ld128<AUX_SC1>(ws, mo + q * 8192, 0); m[2 * q + 1] = ld128<AUX_SC1>(ws, mo + q * 8192 + 16, 0); }
      m[6] = ld128<AUX_SC1>(ws, go, 0); m[7] = ld128<AUX_SC1>(ws, go + 4096, 0);
#pragma unroll
      for (int q = 0; q < 8; ++q) mx = umax4(m[q], mx);
      if (all_true(mx != SENT)) break;
      __builtin_amdgcn_s_sleep(2);
    }
#pragma unroll
    for (int q = 0; q < 8; ++q) {
      const float v0 = asf(m[q >> 2][q & 3]), v1 = asf(m[2 + (q >> 2)][q & 3]), v2 = asf(m[4 + (q >> 2)][q & 3]);
      ga[q] = v0 + dpp<ROR8>(v0); sb[q] = v1 + dpp<ROR8>(v1); cb[q] = 1.f + (v2 + dpp<ROR8>(v2));
    }
#pragma unroll
    for (int q = 0; q < 4; ++q) gb[q] = asf(m[6][q]) + asf(m[7][q]);
  }
  STAMP(7);
  v4u y[4];
  const u32 yo = WS_Y + (u32)(g * 16384 + ((2 * w + (gq >> 1)) * 8 + 4 * rho) * 256 + eb * 128 + (gq & 1) * 64 + kp * 16);
  for (u32 sp = 0; sp < SPIN_CAP; ++sp) {
    cfence();
    u32 mx = 0;
#pragma unroll
    for (int i = 0; i < 4; ++i) y[i] = ld128<AUX_SC1>(ws, yo + i * 256, 0);
#pragma unroll
    for (int i = 0; i < 4; ++i) mx = umax4(y[i], mx);
    if (all_true(mx != SENT)) break;
  }
  STAMP(8);
  float x1[8];
#pragma unroll
  for (int d = 0; d < 4; ++d) {
    float lo = (bflo(y[0][d]) + bflo(y[1][d])) + (bflo(y[2][d]) + bflo(y[3][d]));
    float hi = (bfhi(y[0][d]) + bfhi(y[1][d])) + (bfhi(y[2][d]) + bfhi(y[3][d]));
    lo += dpp<ROR8>(lo); hi += dpp<ROR8>(hi);
    x1[2 * d] = bflo(xv[d]) + ga[2 * d] * lo; x1[2 * d + 1] = bfhi(xv[d]) + ga[2 * d + 1] * hi;
  }
  LAS float* st = (LAS float*)(L + L_ST);
  {
    float s1 = 0.f, s2 = 0.f;
#pragma unroll
    for (int q = 0; q < 8; ++q) { s1 += x1[q]; s2 += x1[q] * x1[q]; }
    float v = rho ? s2 : s1;
    v += dpp<X1>(v); v += dpp<X2>(v); v = swap16_add(v, v); v = swap32_add(v, v);
    if (l < 16 && kp == 0) st[(eb * 2 + rho) * 4 + w] = v;
  }
  *(LAS f32x4*)(L + L_RED + 2048 + (eb * 512 + d0 + 4 * rho) * 4) = rho ? f32x4{x1[4], x1[5], x1[6], x1[7]} : f32x4{x1[0], x1[1], x1[2], x1[3]};
  wg_barrier();
  v4u af;
  {
    const f32x4 s1 = *(const LAS f32x4*)(st + eb * 8), s2 = *(const LAS f32x4*)(st + eb * 8 + 4);
    const float mu = ((s1[0] + s1[1]) + (s1[2] + s1[3])) * (1.f / D);
    const float var = fmaxf(((s2[0] + s2[1]) + (s2[2] + s2[3])) * (1.f / D) - mu * mu, 0.f);
    const float rs = __builtin_amdgcn_rsqf(var + 1e-5f);
    float h2[8], t1 = 0.f, t2 = 0.f;
#pragma unroll
    for (int q = 0; q < 8; ++q) { h2[q] = (x1[q] - mu) * rs * cb[q] + sb[q]; t1 += h2[q]; t2 += h2[q] * h2[q]; }
#pragma unroll
    for (int d = 0; d < 4; ++d) af[d] = rho ? 0x3F803F80u : pk2(h2[2 * d], h2[2 * d + 1]);
    float v = rho ? t2 : t1;
    v += dpp<X1>(v); v += dpp<X2>(v); v = swap16_add(v, v); v = swap32_add(v, v);
    if (l < 16 && kp == 0) st[16 + (eb * 2 + rho) * 4 + w] = v;
  }
  {
    f32x2 t;
#pragma unroll
    for (int i = 0; i < 2; ++i) {
      float ee[4];
#pragma unroll
      for (int k = 0; k < 4; ++k) ee[k] = mfma32(af, f1[k][i], f32x4{0.f, 0.f, 0.f, 0.f})[k];
      t[i] = (ee[0] + ee[1]) + (ee[2] + ee[3]);
    }
    *(LAS f32x2*)(L + L_RED + w * 512 + l * 8) = t;
  }
  const f32x4 x1f = *(const LAS f32x4*)(L + L_RED + 2048 + (fb * 512 + fcol) * 4);
  f32x4 base = {x1f[0] + gb[0] * bflo(b2v.x), x1f[1] + gb[1] * bfhi(b2v.x), x1f[2] + gb[2] * bflo(b2v.y), x1f[3] + gb[3] * bfhi(b2v.y)};
  if (e != 0) base = f32x4{0.f, 0.f, 0.f, 0.f};
  wg_barrier();
  STAMP(9);
  {
    const int ub = l >> 5;
    float T = 0.f, Tw = 0.f;
#pragma unroll
    for (int ww = 0; ww < 4; ++ww) { T += *(const LAS float*)(L + L_RED + ww * 512 + l * 4); Tw += *(const LAS float*)(L + L_RED + ww * 512 + 256 + l * 4); }
    const f32x4 s1 = *(const LAS f32x4*)(st + 16 + ub * 8), s2 = *(const LAS f32x4*)(st + 16 + ub * 8 + 4);
    const float mu = ((s1[0] + s1[1]) + (s1[2] + s1[3])) * (1.f / D);
    const float var = fmaxf(((s2[0] + s2[1]) + (s2[2] + s2[3])) * (1.f / D) - mu * mu, 0.f);
    const float u = (T - mu * Tw) * __builtin_amdgcn_rsqf(var + 1e-5f) + bflo(b1v);
    const float z = u * (1.f + 0.044715f * u * u) * (-2.f * 0.7978845608028654f * 1.4426950408889634f);
    const float h1 = u * __builtin_amdgcn_rcpf(1.f + __builtin_amdgcn_exp2f(z));
    *(LAS u16*)(L + L_QS + w * 128 + l * 2) = (u16)pk2(h1, h1);
  }
  lds_wait();
  const v4u hf = *(const LAS v4u*)(L + L_QS + w * 128 + (c >> 3) * 64 + gq * 16);
  float o[8];
#pragma unroll
  for (int i = 0; i < 8; ++i) o[i] = mfma32(hf, f2[i], f32x4{0.f, 0.f, 0.f, 0.f})[0];
  v4u pv;
#pragma unroll
  for (int q = 0; q < 4; ++q) {
    float lo = o[q], hi = o[4 + q];
    asm volatile("" : "+v"(lo), "+v"(hi));
    pv[q] = unsent(asu(gb[q] * ((gq & 1) ? hi : lo) + base[q]));
  }
  st128(ws, WS_P + (u32)((fb * 16 + 4 * w + (c >> 2)) * 8192 + e * 128 + (gq & 1) * 64 + (c & 3) * 16), pv);
  STAMP(10);
}

__global__ void __launch_bounds__(320, 2) fused_block(Args a) {
  __shared__ __attribute__((aligned(16))) unsigned char lds[L_END];
  LAS unsigned char* L = (LAS unsigned char*)lds;
  const int l = threadIdx.x & 63, w = __builtin_amdgcn_readfirstlane(threadIdx.x >> 6);
  const int g = blockIdx.x & 7, r = blockIdx.x >> 3;
  if (r < 16) role_attn(STAMP_PASS a, L, g, r, w, l);
  else if (r < 24) role_f0(STAMP_PASS a, L, g, r - 16, w, l);
  else role_f1(STAMP_PASS a, L, g, r - 24, w, l);
}

extern "C" void kernel_launch(void* const* d_in, const int* in_sizes, int n_in, void* d_out, int out_size, void* d_ws, size_t ws_size, hipStream_t stream) {
  Args a;
  a.kc = (const u16*)d_in[1]; a.vc = (const u16*)d_in[2]; a.ws = (unsigned char*)d_ws;
  a.wq = (const u16*)d_in[3]; a.wmod = (const u16*)d_in[11]; a.x = (const u16*)d_in[0]; a.c = (const u16*)d_in[10];
  a.wo = (const u16*)d_in[4]; a.w1 = (const u16*)d_in[6]; a.w2 = (const u16*)d_in[8];
  a.b1 = (const u16*)d_in[7]; a.b2 = (const u16*)d_in[9]; a.out = (u16*)d_out;
  fused_block<<<256, 320, 0, stream>>>(a);
}
```
